```python
import torch

def _softmax(x, axis=-1):
    return torch.softmax(x, dim=axis)
def _gelu(x):
    return torch.nn.functional.gelu(x, approximate="tanh")
def _silu(x):
    return torch.nn.functional.silu(x)
def _sigmoid(x):
    return torch.sigmoid(x)
def _layernorm(x, axis=-1, eps=1e-5):
    m = x.mean(dim=axis, keepdim=True)
    v = x.var(dim=axis, keepdim=True, unbiased=False)
    return (x - m) / torch.sqrt(v + eps)
def _scatter_add(tgt, idx, src, axis=0):
    return tgt.clone().index_add_(axis, idx, src)
def _segment_sum(src, seg, n):
    out = torch.zeros((n,) + src.shape[1:], dtype=src.dtype)
    return out.index_add_(0, seg, src)
def _pad(x, pw):
    flat = tuple(v for pair in reversed(pw) for v in pair)
    return torch.nn.functional.pad(x, flat)

kernel_name = 'concat_heads__sliding_block_cached__sliding_block_cached__decode'

def setup_inputs():

    _w = (lambda a: (lambda t: t.to(torch.bfloat16) if t.is_floating_point() else t)(torch.as_tensor(a)))
    def _A_setup():
        B, Sq, D = 4, 1, 512
        Hq, Hkv, Sk, Dh = 32, 8, 8192, 128
        assert Hq % Hkv == 0, (Hq, Hkv)
        return {
            "x": _w((lambda *s: torch.randn(*s, dtype=torch.bfloat16) * 0.8)(B, Sq, D)),
            "k_cache": _w((lambda *s: torch.randn(*s, dtype=torch.bfloat16) * 1.6)(B, Hkv, Sk, Dh)),
            "v_cache": _w((lambda *s: torch.randn(*s, dtype=torch.bfloat16) * 0.2)(B, Hkv, Sk, Dh)),
            "wq": _w((lambda *s: torch.randn(*s, dtype=torch.bfloat16) * 1.6)(D, Hq * Dh) * (D ** -0.5)),
            "wo": _w((lambda *s: torch.randn(*s, dtype=torch.bfloat16) * 0.2)(Hq * Dh, D) * ((Hq * Dh) ** -0.5)),
        }
    _A_in = _A_setup()

    def _B_setup():
        B, Sq, D = 4, 1, 512
        Hq, Hkv, Sk, Dh = 32, 8, 8192, 128
        assert Hq % Hkv == 0, (Hq, Hkv)
        return {
            "x": _w((lambda *s: torch.randn(*s, dtype=torch.bfloat16) * 0.8)(B, Sq, D)),
            "k_cache": _w((lambda *s: torch.randn(*s, dtype=torch.bfloat16) * 1.6)(B, Hkv, Sk, Dh)),
            "v_cache": _w((lambda *s: torch.randn(*s, dtype=torch.bfloat16) * 0.2)(B, Hkv, Sk, Dh)),
            "wq": _w((lambda *s: torch.randn(*s, dtype=torch.bfloat16) * 1.6)(D, Hq * Dh) * (D ** -0.5)),
            "wo": _w((lambda *s: torch.randn(*s, dtype=torch.bfloat16) * 0.2)(Hq * Dh, D) * ((Hq * Dh) ** -0.5)),
        }
    _B_in = _B_setup()

    return {
        "A_x": _A_in["x"],
        "A_k_cache": _A_in["k_cache"],
        "A_v_cache": _A_in["v_cache"],
        "A_wq": _A_in["wq"],
        "A_wo": _A_in["wo"],
        "B_x": _B_in["x"],
        "B_k_cache": _B_in["k_cache"],
        "B_v_cache": _B_in["v_cache"],
        "B_wq": _B_in["wq"],
        "B_wo": _B_in["wo"],
    }

def reference(A_x, A_k_cache, A_v_cache, A_wq, A_wo, B_x, B_k_cache, B_v_cache, B_wq, B_wo):
    def _A_ref(x, k_cache, v_cache, wq, wo):
        B, Sq, D = x.shape
        Hq, Dh = 32, 128
        Hkv, Sk = k_cache.shape[1], k_cache.shape[2]
        W = min(4096, max(256, Sk // 4))
        rep = Hq // Hkv
        q = (lambda x, *d: x.permute(*d))((x @ wq).reshape(B, Sq, Hq, Dh), 0, 2, 1, 3)
        k = (lambda x, n, axis: torch.repeat_interleave(x, n, dim=axis))(k_cache[:, :, Sk - W:, :], rep, axis=1)
        v = (lambda x, n, axis: torch.repeat_interleave(x, n, dim=axis))(v_cache[:, :, Sk - W:, :], rep, axis=1)
        scores = torch.einsum("bhqd,bhkd->bhqk", q, k) * (Dh ** -0.5)
        p = _softmax(scores, axis=-1)
        o = torch.einsum("bhqk,bhkd->bhqd", p, v)
        return (lambda x, *d: x.permute(*d))(o, 0, 2, 1, 3).reshape(B, Sq, Hq * Dh) @ wo

    def _B_ref(x, k_cache, v_cache, wq, wo):
        B, Sq, D = x.shape
        Hq, Dh = 32, 128
        Hkv, Sk = k_cache.shape[1], k_cache.shape[2]
        W = min(4096, max(256, Sk // 4))
        rep = Hq // Hkv
        q = (lambda x, *d: x.permute(*d))((x @ wq).reshape(B, Sq, Hq, Dh), 0, 2, 1, 3)
        k = (lambda x, n, axis: torch.repeat_interleave(x, n, dim=axis))(k_cache[:, :, Sk - W:, :], rep, axis=1)
        v = (lambda x, n, axis: torch.repeat_interleave(x, n, dim=axis))(v_cache[:, :, Sk - W:, :], rep, axis=1)
        scores = torch.einsum("bhqd,bhkd->bhqk", q, k) * (Dh ** -0.5)
        p = _softmax(scores, axis=-1)
        o = torch.einsum("bhqk,bhkd->bhqd", p, v)
        return (lambda x, *d: x.permute(*d))(o, 0, 2, 1, 3).reshape(B, Sq, Hq * Dh) @ wo

    a = _A_ref(A_x, A_k_cache, A_v_cache, A_wq, A_wo)
    b = _B_ref(B_x, B_k_cache, B_v_cache, B_wq, B_wo)
    return torch.concatenate([a, b], axis=-1)
```

```cpp
#include <hip/hip_runtime.h>
#include <hip/hip_bf16.h>
#include <stdint.h>

typedef __attribute__((ext_vector_type(4))) unsigned u32x4;
typedef __attribute__((ext_vector_type(4))) float f32x4;
typedef __attribute__((ext_vector_type(8))) short bf16x8;
typedef unsigned long long u64;
typedef unsigned short u16;

#define RLX_AGENT __ATOMIC_RELAXED, __HIP_MEMORY_SCOPE_AGENT

constexpr int SK = 8192, DH = 128, WIN = 2048;
constexpr int NW = 16;
constexpr unsigned SPIN_CAP = 1u << 22;

constexpr size_t WS_QG  = 512;
constexpr size_t WS_ACC = WS_QG + 2 * 4 * 8 * 256;
constexpr size_t WS_ML  = WS_ACC + 2 * 4 * 8 * 4 * 4 * 128;
constexpr size_t WS_Y   = WS_ML + 2 * 4 * 8 * 4 * 4 * 2;

constexpr int L_QRED = 0;
constexpr int L_QS   = L_QRED + NW * 128;
constexpr int L_FLAG = L_QS + 256;
constexpr int L_PB   = L_FLAG + 4;
constexpr int L_MRG  = L_PB + NW * 128;
constexpr int L_OS   = L_MRG + NW * 4 * 132;
constexpr int L_YRED = L_OS + 512;
constexpr int L_END  = L_YRED + NW * 512;

__device__ __forceinline__ float bf_lo(unsigned u) { return __builtin_bit_cast(float, u << 16); }
__device__ __forceinline__ float bf_hi(unsigned u) { return __builtin_bit_cast(float, u & 0xffff0000u); }
__device__ __forceinline__ unsigned f2bf(float f) {
  return (unsigned)__builtin_bit_cast(u16, __float2bfloat16(f));
}
__device__ __forceinline__ float bfr(float f) { return __builtin_bit_cast(float, f2bf(f) << 16); }

template <int CTRL> __device__ __forceinline__ float dpp(float x) {
  return __builtin_bit_cast(float, __builtin_amdgcn_mov_dpp(__builtin_bit_cast(int, x), CTRL, 0xf, 0xf, true));
}
constexpr int ROR8 = 0x128, ROR4 = 0x124;
__device__ __forceinline__ float xrow16_max(float x) {
  auto s = __builtin_amdgcn_permlane16_swap(__float_as_uint(x), __float_as_uint(x), false, false);
  x = fmaxf(__uint_as_float(s[0]), __uint_as_float(s[1]));
  auto t = __builtin_amdgcn_permlane32_swap(__float_as_uint(x), __float_as_uint(x), false, false);
  return fmaxf(__uint_as_float(t[0]), __uint_as_float(t[1]));
}
__device__ __forceinline__ float xrow16_sum(float x) {
  auto s = __builtin_amdgcn_permlane16_swap(__float_as_uint(x), __float_as_uint(x), false, false);
  x = __uint_as_float(s[0]) + __uint_as_float(s[1]);
  auto t = __builtin_amdgcn_permlane32_swap(__float_as_uint(x), __float_as_uint(x), false, false);
  return __uint_as_float(t[0]) + __uint_as_float(t[1]);
}
__device__ __forceinline__ u32x4 ld_nt(const u16* p) { return __builtin_nontemporal_load(reinterpret_cast<const u32x4*>(p)); }
__device__ __forceinline__ u32x4 ld_pl(const u16* p) { return *reinterpret_cast<const u32x4*>(p); }
__device__ __forceinline__ void put(u64* g, unsigned tag, unsigned v) {
  __hip_atomic_store(g, ((u64)tag << 32) | v, RLX_AGENT);
}
__device__ __forceinline__ u64 get(const u64* g) { return __hip_atomic_load(g, RLX_AGENT); }

__global__ void __launch_bounds__(1024, 4)
fused_decode(const u16* __restrict__ xA, const u16* __restrict__ kA, const u16* __restrict__ vA,
             const u16* __restrict__ wqA, const u16* __restrict__ woA,
             const u16* __restrict__ xB, const u16* __restrict__ kB, const u16* __restrict__ vB,
             const u16* __restrict__ wqB, const u16* __restrict__ woB,
             u16* __restrict__ out, u64* ws) {
  __shared__ __attribute__((aligned(16))) float lds[L_END];
  const int tid = threadIdx.x, lane = tid & 63, wave = __builtin_amdgcn_readfirstlane(tid >> 6);
  const int r = lane >> 4, c = lane & 15;
  const int bid = blockIdx.x, hkv = bid & 7, j = bid >> 3, p = j >> 4, b = (j >> 2) & 3, s = j & 3, jj = j & 15;
  const u16* x  = p ? xB : xA;
  const u16* kc = p ? kB : kA;
  const u16* vc = p ? vB : vA;
  const u16* wq = p ? wqB : wqA;
  const u16* wo = p ? woB : woA;

  const unsigned tag = (unsigned)get(ws + p * 32) + 1u;
  if (tid == 0) __hip_atomic_store(reinterpret_cast<unsigned*>(lds) + L_FLAG, 0u, __ATOMIC_RELAXED, __HIP_MEMORY_SCOPE_WORKGROUP);

  u32x4 wqv[2];
  float xv[2][4];
#pragma unroll
  for (int u = 0; u < 2; ++u) {
    const int k = wave * 32 + u * 16 + (lane >> 2);
    wqv[u] = ld_pl(wq + (size_t)k * 4096 + hkv * 512 + jj * 32 + (lane & 3) * 8);
#pragma unroll
    for (int bb = 0; bb < 4; ++bb) xv[u][bb] = bf_lo(x[bb * 512 + k]);
  }
  const size_t kvbase = ((size_t)(b * 8 + hkv) * SK + (SK - WIN) + s * 512 + wave * 32) * DH;
  u32x4 kf[2][4];
#pragma unroll
  for (int T = 0; T < 2; ++T)
#pragma unroll
    for (int kk = 0; kk < 4; ++kk) kf[T][kk] = ld_nt(kc + kvbase + (size_t)(T * 16 + c) * DH + kk * 32 + r * 8);

  {
    float qa[4][8];
#pragma unroll
    for (int bb = 0; bb < 4; ++bb)
#pragma unroll
      for (int e = 0; e < 8; ++e) qa[bb][e] = 0.f;
#pragma unroll
    for (int u = 0; u < 2; ++u) {
      float wf[8];
#pragma unroll
      for (int e = 0; e < 4; ++e) { wf[2 * e] = bf_lo(wqv[u][e]); wf[2 * e + 1] = bf_hi(wqv[u][e]); }
#pragma unroll
      for (int bb = 0; bb < 4; ++bb)
#pragma unroll
        for (int e = 0; e < 8; ++e) qa[bb][e] = fmaf(xv[u][bb], wf[e], qa[bb][e]);
    }
#pragma unroll
    for (int bb = 0; bb < 4; ++bb)
#pragma unroll
      for (int e = 0; e < 8; ++e) {
        float t = qa[bb][e];
        t += dpp<ROR4>(t); t += dpp<ROR8>(t);
        qa[bb][e] = xrow16_sum(t);
      }
    if (lane < 4) {
#pragma unroll
      for (int bb = 0; bb < 4; ++bb) {
        float* dst = lds + L_QRED + wave * 128 + bb * 32 + lane * 8;
        *reinterpret_cast<f32x4*>(dst)     = f32x4{qa[bb][0], qa[bb][1], qa[bb][2], qa[bb][3]};
        *reinterpret_cast<f32x4*>(dst + 4) = f32x4{qa[bb][4], qa[bb][5], qa[bb][6], qa[bb][7]};
      }
    }
  }
  __syncthreads();
  if (wave == 1) {
    const int bb = lane >> 4, pr = lane & 15;
    float s0 = 0.f, s1 = 0.f;
#pragma unroll
    for (int w = 0; w < NW; ++w) {
      s0 += lds[L_QRED + w * 128 + bb * 32 + 2 * pr];
      s1 += lds[L_QRED + w * 128 + bb * 32 + 2 * pr + 1];
    }
    put(ws + WS_QG + ((size_t)(p * 4 + bb) * 8 + hkv) * 256 + jj * 16 + pr, tag, f2bf(s0) | (f2bf(s1) << 16));
  }
  if (wave == 0) {
    const u64* src = ws + WS_QG + ((size_t)(p * 4 + b) * 8 + hkv) * 256 + lane * 4;
    u64 g0, g1, g2, g3;
    for (unsigned spins = 0;; ++spins) {
      g0 = get(src); g1 = get(src + 1); g2 = get(src + 2); g3 = get(src + 3);
      const bool ok = ((unsigned)(g0 >> 32) == tag) & ((unsigned)(g1 >> 32) == tag) &
                      ((unsigned)(g2 >> 32) == tag) & ((unsigned)(g3 >> 32) == tag);
      if (__all(ok) || spins > SPIN_CAP) break;
      __builtin_amdgcn_s_sleep(1);
    }
    *reinterpret_cast<u32x4*>(lds + L_QS + lane * 4) = u32x4{(unsigned)g0, (unsigned)g1, (unsigned)g2, (unsigned)g3};
    __builtin_amdgcn_fence(__ATOMIC_RELEASE, "workgroup");
    if (lane == 0) __hip_atomic_store(reinterpret_cast<unsigned*>(lds) + L_FLAG, 1u, __ATOMIC_RELAXED, __HIP_MEMORY_SCOPE_WORKGROUP);
  }
#pragma unroll
  for (int T = 0; T < 2; ++T)
#pragma unroll
    for (int kk = 0; kk < 4; ++kk) asm volatile("" : "+v"(kf[T][kk]));
  u32x4 vf4[8];
#pragma unroll
  for (int i = 0; i < 8; ++i) vf4[i] = ld_nt(vc + kvbase + (size_t)(4 * i + r) * DH + c * 8);
  const int g_o = j & 3, nq = (j >> 2) & 3, h_o = hkv * 4 + g_o;
  u32x4 wov[2];
#pragma unroll
  for (int u = 0; u < 2; ++u) wov[u] = ld_nt(wo + (size_t)(h_o * 128 + wave * 8 + u * 4 + r) * 512 + nq * 128 + c * 8);

  while (__hip_atomic_load(reinterpret_cast<unsigned*>(lds) + L_FLAG, __ATOMIC_RELAXED, __HIP_MEMORY_SCOPE_WORKGROUP) == 0u)
    __builtin_amdgcn_s_sleep(1);
  __builtin_amdgcn_fence(__ATOMIC_ACQUIRE, "workgroup");

  float m, l;
  {
    bf16x8 qf[4];
#pragma unroll
    for (int kk = 0; kk < 4; ++kk) {
      u32x4 t = *reinterpret_cast<const u32x4*>(lds + L_QS + (c & 3) * 64 + kk * 16 + r * 4);
      if (c >= 4) t = u32x4{0u, 0u, 0u, 0u};
      qf[kk] = __builtin_bit_cast(bf16x8, t);
    }
    f32x4 sc[2];
#pragma unroll
    for (int T = 0; T < 2; ++T) {
      sc[T] = f32x4{0.f, 0.f, 0.f, 0.f};
#pragma unroll
      for (int kk = 0; kk < 4; ++kk)
        sc[T] = __builtin_amdgcn_mfma_f32_16x16x32_bf16(__builtin_bit_cast(bf16x8, kf[T][kk]), qf[kk], sc[T], 0, 0, 0);
    }
    float t[8];
#pragma unroll
    for (int T = 0; T < 2; ++T)
#pragma unroll
      for (int e = 0; e < 4; ++e)
        t[T * 4 + e] = bfr(bfr(sc[T][e]) * 0.08838834764831845f) * 1.4426950408889634f;
    m = t[0];
#pragma unroll
    for (int e = 1; e < 8; ++e) m = fmaxf(m, t[e]);
    m = xrow16_max(m);
    l = 0.f;
#pragma unroll
    for (int e = 0; e < 8; ++e) { t[e] = __builtin_amdgcn_exp2f(t[e] - m); l += t[e]; }
    l = xrow16_sum(l);
    if (c < 4) {
#pragma unroll
      for (int T = 0; T < 2; ++T)
#pragma unroll
        for (int e = 0; e < 4; ++e) lds[L_PB + wave * 128 + (T * 16 + r * 4 + e) * 4 + c] = t[T * 4 + e];
    }
  }
  __builtin_amdgcn_wave_barrier();
  {
    float acc[4][8];
#pragma unroll
    for (int g = 0; g < 4; ++g)
#pragma unroll
      for (int e = 0; e < 8; ++e) acc[g][e] = 0.f;
#pragma unroll
    for (int i = 0; i < 8; ++i) {
      const f32x4 pr = *reinterpret_cast<const f32x4*>(lds + L_PB + wave * 128 + (4 * i + r) * 4);
      float vf[8];
#pragma unroll
      for (int e = 0; e < 4; ++e) { vf[2 * e] = bf_lo(vf4[i][e]); vf[2 * e + 1] = bf_hi(vf4[i][e]); }
#pragma unroll
      for (int g = 0; g < 4; ++g)
#pragma unroll
        for (int e = 0; e < 8; ++e) acc[g][e] = fmaf(pr[g], vf[e], acc[g][e]);
    }
#pragma unroll
    for (int g = 0; g < 4; ++g)
#pragma unroll
      for (int e = 0; e < 8; ++e) acc[g][e] = xrow16_sum(acc[g][e]);
    if (r == 0) {
#pragma unroll
      for (int g = 0; g < 4; ++g) {
        float* rec = lds + L_MRG + (wave * 4 + g) * 132;
        *reinterpret_cast<f32x4*>(rec + 4 + 8 * c)     = f32x4{acc[g][0], acc[g][1], acc[g][2], acc[g][3]};
        *reinterpret_cast<f32x4*>(rec + 4 + 8 * c + 4) = f32x4{acc[g][4], acc[g][5], acc[g][6], acc[g][7]};
      }
      if (c < 4) { float* rec = lds + L_MRG + (wave * 4 + c) * 132; rec[0] = m; rec[1] = l; }
    }
  }
  __syncthreads();
  if (tid < 512) {
    const int g = tid >> 7, d = tid & 127;
    float M = -INFINITY;
#pragma unroll
    for (int w = 0; w < NW; ++w) M = fmaxf(M, lds[L_MRG + (w * 4 + g) * 132]);
    float L = 0.f, o = 0.f;
#pragma unroll
    for (int w = 0; w < NW; ++w) {
      const float* rec = lds + L_MRG + (w * 4 + g) * 132;
      const float wt = __builtin_amdgcn_exp2f(rec[0] - M);
      L = fmaf(wt, rec[1], L); o = fmaf(wt, rec[4 + d], o);
    }
    const size_t slot = (((size_t)(p * 4 + b) * 8 + hkv) * 4 + s) * 4 + g;
    put(ws + WS_ACC + slot * 128 + d, tag, __float_as_uint(o));
    if (d == 0) { put(ws + WS_ML + slot * 2, tag, __float_as_uint(M)); put(ws + WS_ML + slot * 2 + 1, tag, __float_as_uint(L)); }

    const int bb = tid >> 7;
    u64 a[4], mm[4], ll[4];
    for (unsigned spins = 0;; ++spins) {
      bool ok = true;
#pragma unroll
      for (int ss = 0; ss < 4; ++ss) {
        const size_t sl = (((size_t)(p * 4 + bb) * 8 + hkv) * 4 + ss) * 4 + g_o;
        a[ss] = get(ws + WS_ACC + sl * 128 + d); mm[ss] = get(ws + WS_ML + sl * 2); ll[ss] = get(ws + WS_ML + sl * 2 + 1);
      }
#pragma unroll
      for (int ss = 0; ss < 4; ++ss)
        ok &= ((unsigned)(a[ss] >> 32) == tag) & ((unsigned)(mm[ss] >> 32) == tag) & ((unsigned)(ll[ss] >> 32) == tag);
      if (__all(ok) || spins > SPIN_CAP) break;
      __builtin_amdgcn_s_sleep(1);
    }
    float M2 = -INFINITY;
#pragma unroll
    for (int ss = 0; ss < 4; ++ss) M2 = fmaxf(M2, __uint_as_float((unsigned)mm[ss]));
    float L2 = 0.f, o2 = 0.f;
#pragma unroll
    for (int ss = 0; ss < 4; ++ss) {
      const float wt = __builtin_amdgcn_exp2f(__uint_as_float((unsigned)mm[ss]) - M2);
      L2 = fmaf(wt, __uint_as_float((unsigned)ll[ss]), L2); o2 = fmaf(wt, __uint_as_float((unsigned)a[ss]), o2);
    }
    lds[L_OS + bb * 128 + d] = bfr(o2 / L2);
  }
  __syncthreads();
  {
    float y[4][8];
#pragma unroll
    for (int bb = 0; bb < 4; ++bb)
#pragma unroll
      for (int e = 0; e < 8; ++e) y[bb][e] = 0.f;
#pragma unroll
    for (int u = 0; u < 2; ++u) {
      float wf[8];
#pragma unroll
      for (int e = 0; e < 4; ++e) { wf[2 * e] = bf_lo(wov[u][e]); wf[2 * e + 1] = bf_hi(wov[u][e]); }
#pragma unroll
      for (int bb = 0; bb < 4; ++bb) {
        const float ov = lds[L_OS + bb * 128 + wave * 8 + u * 4 + r];
#pragma unroll
        for (int e = 0; e < 8; ++e) y[bb][e] = fmaf(ov, wf[e], y[bb][e]);
      }
    }
#pragma unroll
    for (int bb = 0; bb < 4; ++bb)
#pragma unroll
      for (int e = 0; e < 8; ++e) y[bb][e] = xrow16_sum(y[bb][e]);
    if (r == 0) {
#pragma unroll
      for (int bb = 0; bb < 4; ++bb) {
        float* dst = lds + L_YRED + wave * 512 + bb * 128 + 8 * c;
        *reinterpret_cast<f32x4*>(dst)     = f32x4{y[bb][0], y[bb][1], y[bb][2], y[bb][3]};
        *reinterpret_cast<f32x4*>(dst + 4) = f32x4{y[bb][4], y[bb][5], y[bb][6], y[bb][7]};
      }
    }
  }
  __syncthreads();
  const int idx = jj * 8 + hkv;
  if (tid < 512) {
    float sum = 0.f;
#pragma unroll
    for (int w = 0; w < NW; ++w) sum += lds[L_YRED + w * 512 + tid];
    put(ws + WS_Y + ((size_t)(p * 32 + h_o) * 4 + (tid >> 7)) * 512 + nq * 128 + (tid & 127), tag, __float_as_uint(sum));

    const int br = idx >> 5, n0 = (idx & 31) * 16, hh = tid & 31, n = tid >> 5;
    const u64* src = ws + WS_Y + ((size_t)(p * 32 + hh) * 4 + br) * 512 + n0 + n;
    u64 g;
    for (unsigned spins = 0;; ++spins) {
      g = get(src);
      if (__all((unsigned)(g >> 32) == tag) || spins > SPIN_CAP) break;
      __builtin_amdgcn_s_sleep(1);
    }
    float v = __uint_as_float((unsigned)g);
    v += __shfl_xor(v, 1); v += __shfl_xor(v, 2); v += __shfl_xor(v, 4); v += __shfl_xor(v, 8); v += __shfl_xor(v, 16);
    if (hh == 0) out[br * 1024 + p * 512 + n0 + n] = (u16)f2bf(v);
  }
  if (idx == 0) {
    __syncthreads();
    if (tid == 0) __hip_atomic_store(ws + p * 32, (u64)tag, RLX_AGENT);
  }
}

extern "C" void kernel_launch(void* const* d_in, const int* in_sizes, int n_in,
                              void* d_out, int out_size, void* d_ws, size_t ws_size,
                              hipStream_t stream) {
  fused_decode<<<256, 1024, 0, stream>>>((const u16*)d_in[0], (const u16*)d_in[1], (const u16*)d_in[2], (const u16*)d_in[3],
                                        (const u16*)d_in[4], (const u16*)d_in[5], (const u16*)d_in[6], (const u16*)d_in[7],
                                        (const u16*)d_in[8], (const u16*)d_in[9], (u16*)d_out, (u64*)d_ws);
}
```

```cpp
#include <hip/hip_runtime.h>
#include <hip/hip_bf16.h>
#include <stdint.h>

typedef __attribute__((ext_vector_type(4))) unsigned u32x4;
typedef __attribute__((ext_vector_type(4))) float f32x4;
typedef __attribute__((ext_vector_type(8))) short bf16x8;
typedef unsigned long long u64;
typedef unsigned short u16;

#define RLX_AGENT __ATOMIC_RELAXED, __HIP_MEMORY_SCOPE_AGENT

constexpr int SK = 8192, DH = 128, WIN = 2048;
constexpr int NW = 16;
constexpr unsigned SPIN_CAP = 1u << 22;

constexpr size_t WS_QG  = 512;
constexpr size_t WS_ACC = WS_QG + 2 * 4 * 8 * 256;
constexpr size_t WS_ML  = WS_ACC + 2 * 4 * 8 * 4 * 4 * 128;
constexpr size_t WS_Y   = WS_ML + 2 * 4 * 8 * 4 * 4 * 2;

constexpr int L_IMG  = 0;
constexpr int L_QRED = 131072;
constexpr int L_QS   = L_QRED + 8192;
constexpr int L_FLAG = L_QS + 1024;
constexpr int L_WMAX = L_FLAG + 16;
constexpr int L_LSUM = L_WMAX + 256;
constexpr int L_P    = L_LSUM + 256;
constexpr int L_OS   = L_P + 4 * 1040;
constexpr int L_END  = L_OS + 4 * 272;

typedef __attribute__((ext_vector_type(4))) short s16x4;
typedef __attribute__((ext_vector_type(2))) unsigned u32x2;
typedef __attribute__((address_space(3))) s16x4 lds_s16x4;
__device__ __forceinline__ unsigned off_b(unsigned row, unsigned ch) { return 256u * row + 16u * (ch ^ (((row & 3) << 2) | ((row >> 2) & 3))); }

__device__ __forceinline__ float bf_lo(unsigned u) { return __builtin_bit_cast(float, u << 16); }
__device__ __forceinline__ float bf_hi(unsigned u) { return __builtin_bit_cast(float, u & 0xffff0000u); }
__device__ __forceinline__ unsigned f2bf(float f) {
  return (unsigned)__builtin_bit_cast(u16, __float2bfloat16(f));
}
__device__ __forceinline__ float bfr(float f) { return __builtin_bit_cast(float, f2bf(f) << 16); }

template <int CTRL> __device__ __forceinline__ float dpp(float x) {
  return __builtin_bit_cast(float, __builtin_amdgcn_mov_dpp(__builtin_bit_cast(int, x), CTRL, 0xf, 0xf, true));
}
constexpr int ROR8 = 0x128, ROR4 = 0x124;
__device__ __forceinline__ float xrow16_max(float x) {
  auto s = __builtin_amdgcn_permlane16_swap(__float_as_uint(x), __float_as_uint(x), false, false);
  x = fmaxf(__uint_as_float(s[0]), __uint_as_float(s[1]));
  auto t = __builtin_amdgcn_permlane32_swap(__float_as_uint(x), __float_as_uint(x), false, false);
  return fmaxf(__uint_as_float(t[0]), __uint_as_float(t[1]));
}
__device__ __forceinline__ float xrow16_sum(float x) {
  auto s = __builtin_amdgcn_permlane16_swap(__float_as_uint(x), __float_as_uint(x), false, false);
  x = __uint_as_float(s[0]) + __uint_as_float(s[1]);
  auto t = __builtin_amdgcn_permlane32_swap(__float_as_uint(x), __float_as_uint(x), false, false);
  return __uint_as_float(t[0]) + __uint_as_float(t[1]);
}
__device__ __forceinline__ u32x4 ld_nt(const u16* p) { return __builtin_nontemporal_load(reinterpret_cast<const u32x4*>(p)); }
__device__ __forceinline__ u32x4 ld_pl(const u16* p) { return *reinterpret_cast<const u32x4*>(p); }
__device__ __forceinline__ void put(u64* g, unsigned tag, unsigned v) {
  __hip_atomic_store(g, ((u64)tag << 32) | v, RLX_AGENT);
}
__device__ __forceinline__ u64 get(const u64* g) { return __hip_atomic_load(g, RLX_AGENT); }

#define STAMP(k) do { } while (0)

__global__ void __launch_bounds__(1024, 4)
fused_decode(const u16* __restrict__ xA, const u16* __restrict__ kA, const u16* __restrict__ vA,
             const u16* __restrict__ wqA, const u16* __restrict__ woA,
             const u16* __restrict__ xB, const u16* __restrict__ kB, const u16* __restrict__ vB,
             const u16* __restrict__ wqB, const u16* __restrict__ woB,
             u16* __restrict__ out, u64* ws) {
  __shared__ __attribute__((aligned(16))) char lds[L_END];
  const int tid = threadIdx.x, lane = tid & 63, wave = __builtin_amdgcn_readfirstlane(tid >> 6);
  const int r = lane >> 4, c = lane & 15, tq = c >> 2, tp = c & 3;
  const int bid = blockIdx.x, hkv = bid & 7, j = bid >> 3, p = j >> 4, b = (j >> 2) & 3, s = j & 3, jj = j & 15;
  const u16* x  = p ? xB : xA;
  const u16* kc = p ? kB : kA;
  const u16* vc = p ? vB : vA;
  const u16* wq = p ? wqB : wqA;
  const u16* wo = p ? woB : woA;
  float* qred = reinterpret_cast<float*>(lds + L_QRED);
  unsigned* flag = reinterpret_cast<unsigned*>(lds + L_FLAG);
  float* wmax = reinterpret_cast<float*>(lds + L_WMAX);
  float* lsum = reinterpret_cast<float*>(lds + L_LSUM);
#define TRREAD(byteoff) __builtin_amdgcn_ds_read_tr16_b64_v4i16((lds_s16x4*)(lds + (byteoff)))
#define CAT8(lo, hi) bf16x8{lo[0], lo[1], lo[2], lo[3], hi[0], hi[1], hi[2], hi[3]}

  const unsigned tag = reinterpret_cast<const unsigned*>(ws)[p * 64] + 1u;
  if (tid == 0) __hip_atomic_store(flag, 0u, __ATOMIC_RELAXED, __HIP_MEMORY_SCOPE_WORKGROUP);
  STAMP(0);
  u32x4 wqv[2];
#pragma unroll
  for (int u = 0; u < 2; ++u)
    wqv[u] = ld_nt(wq + (size_t)(wave * 32 + u * 16 + (lane >> 2)) * 4096 + hkv * 512 + jj * 32 + (lane & 3) * 8);
  u32x4 xa = ld_pl(x + (c & 3) * 512 + wave * 32 + r * 8);
  __syncthreads();
  const size_t kvbase = ((size_t)(b * 8 + hkv) * SK + (SK - WIN) + s * 512 + wave * 32) * DH;
  const u16* kp = kc + kvbase + (size_t)c * DH + r * 8;
  const u16* vp = vc + kvbase + (size_t)r * DH + c * 8;
  const int g_o = j & 3, nq = (j >> 2) & 3, h_o = hkv * 4 + g_o;
  const u16* wop = wo + (size_t)(h_o * 128 + wave * 8 + r) * 512 + nq * 128 + c * 8;
  u32x4 kf[2][4];
  u32x4 vf4[8];
  u32x4 wov[2];
#pragma unroll
  for (int T = 0; T < 2; ++T)
#pragma unroll
    for (int kk = 0; kk < 4; ++kk) kf[T][kk] = ld_nt(kp + T * 16 * DH + kk * 32);
  {
#pragma unroll
    for (int u = 0; u < 2; ++u)
      *reinterpret_cast<u32x4*>(lds + L_IMG + (wave * 32 + u * 16 + (lane >> 2)) * 64 + (lane & 3) * 16) = wqv[u];
    if (c >= 4) xa = u32x4{0u, 0u, 0u, 0u};
    __builtin_amdgcn_wave_barrier();
#pragma unroll
    for (int ct = 0; ct < 2; ++ct) {
      const s16x4 lo = TRREAD(L_IMG + (wave * 32 + 8 * r + tq) * 64 + ct * 32 + tp * 8);
      const s16x4 hi = TRREAD(L_IMG + (wave * 32 + 8 * r + 4 + tq) * 64 + ct * 32 + tp * 8);
      const f32x4 d = __builtin_amdgcn_mfma_f32_16x16x32_bf16(__builtin_bit_cast(bf16x8, xa), CAT8(lo, hi), f32x4{0.f, 0.f, 0.f, 0.f}, 0, 0, 0);
      if (lane < 16) {
#pragma unroll
        for (int bb = 0; bb < 4; ++bb) qred[wave * 128 + bb * 32 + ct * 16 + lane] = d[bb];
      }
    }
  }
  STAMP(1);
  __syncthreads();
  if (wave == 1) {
    const int bb = lane >> 4, pr = lane & 15;
    float s0 = 0.f, s1 = 0.f;
#pragma unroll
    for (int w = 0; w < NW; ++w) {
      const float2 t2 = *reinterpret_cast<const float2*>(qred + w * 128 + bb * 32 + 2 * pr);
      s0 += t2.x; s1 += t2.y;
    }
    put(ws + WS_QG + ((size_t)(p * 4 + bb) * 8 + hkv) * 256 + jj * 16 + pr, tag, f2bf(s0) | (f2bf(s1) << 16));
  }
  if (wave == 0) {
    const u64* src = ws + WS_QG + ((size_t)(p * 4 + b) * 8 + hkv) * 256 + lane * 4;
    u64 g0, g1, g2, g3;
    for (unsigned spins = 0;; ++spins) {
      g0 = get(src); g1 = get(src + 1); g2 = get(src + 2); g3 = get(src + 3);
      const bool ok = ((unsigned)(g0 >> 32) == tag) & ((unsigned)(g1 >> 32) == tag) &
                      ((unsigned)(g2 >> 32) == tag) & ((unsigned)(g3 >> 32) == tag);
      if (__all(ok) || spins > SPIN_CAP) break;
      __builtin_amdgcn_s_sleep(1);
    }
    *reinterpret_cast<u32x4*>(lds + L_QS + lane * 16) = u32x4{(unsigned)g0, (unsigned)g1, (unsigned)g2, (unsigned)g3};
    __builtin_amdgcn_fence(__ATOMIC_RELEASE, "workgroup");
    if (lane == 0) __hip_atomic_store(flag, 1u, __ATOMIC_RELAXED, __HIP_MEMORY_SCOPE_WORKGROUP);
  }
  asm volatile("s_waitcnt vmcnt(0)" ::: "memory");
  STAMP(2);
#pragma unroll
  for (int i = 0; i < 8; ++i) vf4[i] = ld_nt(vp + (size_t)(4 * i) * DH);
#pragma unroll
  for (int u = 0; u < 2; ++u) wov[u] = ld_nt(wop + (size_t)(u * 4) * 512);

  while (__hip_atomic_load(flag, __ATOMIC_RELAXED, __HIP_MEMORY_SCOPE_WORKGROUP) == 0u) __builtin_amdgcn_s_sleep(1);
  __builtin_amdgcn_fence(__ATOMIC_ACQUIRE, "workgroup");
  STAMP(3);
  float t[8];
  {
    bf16x8 qf[4];
#pragma unroll
    for (int kk = 0; kk < 4; ++kk) {
      u32x4 q4 = *reinterpret_cast<const u32x4*>(lds + L_QS + (c & 3) * 256 + kk * 64 + r * 16);
      if (c >= 4) q4 = u32x4{0u, 0u, 0u, 0u};
      qf[kk] = __builtin_bit_cast(bf16x8, q4);
    }
#pragma unroll
    for (int T = 0; T < 2; ++T) {
      f32x4 sc = f32x4{0.f, 0.f, 0.f, 0.f};
#pragma unroll
      for (int kk = 0; kk < 4; ++kk)
        sc = __builtin_amdgcn_mfma_f32_16x16x32_bf16(__builtin_bit_cast(bf16x8, kf[T][kk]), qf[kk], sc, 0, 0, 0);
#pragma unroll
      for (int e = 0; e < 4; ++e) t[T * 4 + e] = bfr(bfr(sc[e]) * 0.08838834764831845f) * 1.4426950408889634f;
    }
    float m = t[0];
#pragma unroll
    for (int e = 1; e < 8; ++e) m = fmaxf(m, t[e]);
    m = xrow16_max(m);
    if (lane < 4) wmax[wave * 4 + lane] = m;
  }
  STAMP(4);
  __syncthreads();
  float M = wmax[c & 3];
#pragma unroll
  for (int w = 1; w < NW; ++w) M = fmaxf(M, wmax[w * 4 + (c & 3)]);
  {
    float l = 0.f;
#pragma unroll
    for (int e = 0; e < 8; ++e) { t[e] = __builtin_amdgcn_exp2f(t[e] - M); l += t[e]; }
    l = xrow16_sum(l);
    if (lane < 4) lsum[wave * 4 + lane] = l;
    if (c < 4) {
#pragma unroll
      for (int T = 0; T < 2; ++T)
        *reinterpret_cast<u32x2*>(lds + L_P + c * 1040 + (wave * 32 + T * 16 + r * 4) * 2) =
            u32x2{f2bf(t[T * 4]) | (f2bf(t[T * 4 + 1]) << 16), f2bf(t[T * 4 + 2]) | (f2bf(t[T * 4 + 3]) << 16)};
    }
#pragma unroll
    for (int i = 0; i < 8; ++i) *reinterpret_cast<u32x4*>(lds + L_IMG + off_b(wave * 32 + 4 * i + r, c)) = vf4[i];
  }
  STAMP(5);
  __syncthreads();
  if (wave < 8) {
    f32x4 o = f32x4{0.f, 0.f, 0.f, 0.f};
#pragma unroll
    for (int ks = 0; ks < 16; ++ks) {
      u32x4 pa = *reinterpret_cast<const u32x4*>(lds + L_P + (c & 3) * 1040 + (ks * 32 + r * 8) * 2);
      if (c >= 4) pa = u32x4{0u, 0u, 0u, 0u};
      const s16x4 lo = TRREAD(L_IMG + off_b(ks * 32 + 8 * r + tq, 2 * wave + (tp >> 1)) + 8 * (tp & 1));
      const s16x4 hi = TRREAD(L_IMG + off_b(ks * 32 + 8 * r + 4 + tq, 2 * wave + (tp >> 1)) + 8 * (tp & 1));
      o = __builtin_amdgcn_mfma_f32_16x16x32_bf16(__builtin_bit_cast(bf16x8, pa), CAT8(lo, hi), o, 0, 0, 0);
    }
    const size_t slot = (((size_t)(p * 4 + b) * 8 + hkv) * 4 + s) * 4;
    if (lane < 16) {
#pragma unroll
      for (int g = 0; g < 4; ++g) put(ws + WS_ACC + (slot + g) * 128 + wave * 16 + lane, tag, __float_as_uint(o[g]));
    }
    if (wave == 0 && lane < 4) {
      float L = 0.f;
#pragma unroll
      for (int w = 0; w < NW; ++w) L += lsum[w * 4 + lane];
      put(ws + WS_ML + (slot + lane) * 2, tag, __float_as_uint(M));
      put(ws + WS_ML + (slot + lane) * 2 + 1, tag, __float_as_uint(L));
    }
    STAMP(6);
  } else {
    const int bb = (tid - 512) >> 7, d = tid & 127;
    u64 a[4], mm[4], ll[4];
    for (unsigned spins = 0;; ++spins) {
      bool ok = true;
#pragma unroll
      for (int ss = 0; ss < 4; ++ss) {
        const size_t sl = (((size_t)(p * 4 + bb) * 8 + hkv) * 4 + ss) * 4 + g_o;
        a[ss] = get(ws + WS_ACC + sl * 128 + d); mm[ss] = get(ws + WS_ML + sl * 2); ll[ss] = get(ws + WS_ML + sl * 2 + 1);
      }
#pragma unroll
      for (int ss = 0; ss < 4; ++ss)
        ok &= ((unsigned)(a[ss] >> 32) == tag) & ((unsigned)(mm[ss] >> 32) == tag) & ((unsigned)(ll[ss] >> 32) == tag);
      if (__all(ok) || spins > SPIN_CAP) break;
      __builtin_amdgcn_s_sleep(1);
    }
    STAMP(7);
    float M2 = -INFINITY;
#pragma unroll
    for (int ss = 0; ss < 4; ++ss) M2 = fmaxf(M2, __uint_as_float((unsigned)mm[ss]));
    float L2 = 0.f, o2 = 0.f;
#pragma unroll
    for (int ss = 0; ss < 4; ++ss) {
      const float wt = __builtin_amdgcn_exp2f(__uint_as_float((unsigned)mm[ss]) - M2);
      L2 = fmaf(wt, __uint_as_float((unsigned)ll[ss]), L2); o2 = fmaf(wt, __uint_as_float((unsigned)a[ss]), o2);
    }
    *reinterpret_cast<u16*>(lds + L_OS + bb * 272 + d * 2) = (u16)f2bf(o2 / L2);
  }
  __syncthreads();
#pragma unroll
  for (int u = 0; u < 2; ++u) *reinterpret_cast<u32x4*>(lds + L_IMG + off_b(wave * 8 + u * 4 + r, c)) = wov[u];
  __syncthreads();
  const int idx = jj * 8 + hkv;
  if (wave < 8) {
    f32x4 y = f32x4{0.f, 0.f, 0.f, 0.f};
#pragma unroll
    for (int ks = 0; ks < 4; ++ks) {
      u32x4 oa = *reinterpret_cast<const u32x4*>(lds + L_OS + (c & 3) * 272 + (ks * 32 + r * 8) * 2);
      if (c >= 4) oa = u32x4{0u, 0u, 0u, 0u};
      const s16x4 lo = TRREAD(L_IMG + off_b(ks * 32 + 8 * r + tq, 2 * wave + (tp >> 1)) + 8 * (tp & 1));
      const s16x4 hi = TRREAD(L_IMG + off_b(ks * 32 + 8 * r + 4 + tq, 2 * wave + (tp >> 1)) + 8 * (tp & 1));
      y = __builtin_amdgcn_mfma_f32_16x16x32_bf16(__builtin_bit_cast(bf16x8, oa), CAT8(lo, hi), y, 0, 0, 0);
    }
    if (lane < 16) {
#pragma unroll
      for (int bb = 0; bb < 4; ++bb)
        put(ws + WS_Y + ((size_t)(p * 32 + h_o) * 4 + bb) * 512 + nq * 128 + wave * 16 + lane, tag, __float_as_uint(y[bb]));
    }
    STAMP(8);
  } else {
    const int t2 = tid - 512, br = idx >> 5, n0 = (idx & 31) * 16, hh = t2 & 31, n = t2 >> 5;
    const u64* src = ws + WS_Y + ((size_t)(p * 32 + hh) * 4 + br) * 512 + n0 + n;
    u64 g;
    for (unsigned spins = 0;; ++spins) {
      g = get(src);
      if (__all((unsigned)(g >> 32) == tag) || spins > SPIN_CAP) break;
      __builtin_amdgcn_s_sleep(1);
    }
    STAMP(9);
    float v = __uint_as_float((unsigned)g);
    v += __shfl_xor(v, 1); v += __shfl_xor(v, 2); v += __shfl_xor(v, 4); v += __shfl_xor(v, 8); v += __shfl_xor(v, 16);
    if (hh == 0) out[br * 1024 + p * 512 + n0 + n] = (u16)f2bf(v);
  }
  if (idx == 0) {
    __syncthreads();
    if (tid == 0) __hip_atomic_store(ws + p * 32, (u64)tag, RLX_AGENT);
  }
}

extern "C" void kernel_launch(void* const* d_in, const int* in_sizes, int n_in,
                              void* d_out, int out_size, void* d_ws, size_t ws_size,
                              hipStream_t stream) {
  fused_decode<<<256, 1024, 0, stream>>>((const u16*)d_in[0], (const u16*)d_in[1], (const u16*)d_in[2], (const u16*)d_in[3],
                                        (const u16*)d_in[4], (const u16*)d_in[5], (const u16*)d_in[6], (const u16*)d_in[7],
                                        (const u16*)d_in[8], (const u16*)d_in[9], (u16*)d_out, (u64*)d_ws);
}
```

```cpp
#include <hip/hip_runtime.h>
#include <hip/hip_bf16.h>
#include <stdint.h>

typedef __attribute__((ext_vector_type(4))) unsigned u32x4;
typedef __attribute__((ext_vector_type(4))) float f32x4;
typedef __attribute__((ext_vector_type(8))) short bf16x8;
typedef unsigned long long u64;
typedef unsigned short u16;

#define XMODE 1
#define RLX_AGENT __ATOMIC_RELAXED, __HIP_MEMORY_SCOPE_AGENT

constexpr int SK = 8192, DH = 128, WIN = 2048;
constexpr int NW = 16;
constexpr unsigned SPIN_CAP = 1u << 22;

constexpr size_t WS_QG  = 512;
constexpr size_t WS_ACC = WS_QG + 2 * 4 * 8 * 1024;
constexpr size_t WS_ML  = WS_ACC + 2 * 4 * 8 * 4 * 4 * 128;
constexpr size_t WS_Y   = WS_ML + 2 * 4 * 8 * 4 * 4 * 2;

constexpr int L_IMG  = 0;
constexpr int L_QRED = 131072;
constexpr int L_QS   = L_QRED + 16384;
constexpr int L_FLAG = L_QS + 1024;
constexpr int L_WMAX = L_FLAG + 16;
constexpr int L_LSUM = L_WMAX + 256;
constexpr int L_P    = L_LSUM + 256;
constexpr int L_OS   = L_P + 4 * 1040;
constexpr int L_END  = L_OS + 4 * 272;

typedef __attribute__((ext_vector_type(4))) short s16x4;
typedef __attribute__((ext_vector_type(2))) unsigned u32x2;
typedef __attribute__((address_space(3))) s16x4 lds_s16x4;
__device__ __forceinline__ unsigned off_b(unsigned row, unsigned ch) { return 256u * row + 16u * (ch ^ (((row & 3) << 2) | ((row >> 2) & 3))); }

__device__ __forceinline__ float bf_lo(unsigned u) { return __builtin_bit_cast(float, u << 16); }
__device__ __forceinline__ float bf_hi(unsigned u) { return __builtin_bit_cast(float, u & 0xffff0000u); }
__device__ __forceinline__ unsigned f2bf(float f) {
  return (unsigned)__builtin_bit_cast(u16, __float2bfloat16(f));
}
__device__ __forceinline__ float bfr(float f) { return __builtin_bit_cast(float, f2bf(f) << 16); }

template <int CTRL> __device__ __forceinline__ float dpp(float x) {
  return __builtin_bit_cast(float, __builtin_amdgcn_mov_dpp(__builtin_bit_cast(int, x), CTRL, 0xf, 0xf, true));
}
constexpr int ROR8 = 0x128, ROR4 = 0x124;
__device__ __forceinline__ float xrow16_max(float x) {
  auto s = __builtin_amdgcn_permlane16_swap(__float_as_uint(x), __float_as_uint(x), false, false);
  x = fmaxf(__uint_as_float(s[0]), __uint_as_float(s[1]));
  auto t = __builtin_amdgcn_permlane32_swap(__float_as_uint(x), __float_as_uint(x), false, false);
  return fmaxf(__uint_as_float(t[0]), __uint_as_float(t[1]));
}
__device__ __forceinline__ float xrow16_sum(float x) {
  auto s = __builtin_amdgcn_permlane16_swap(__float_as_uint(x), __float_as_uint(x), false, false);
  x = __uint_as_float(s[0]) + __uint_as_float(s[1]);
  auto t = __builtin_amdgcn_permlane32_swap(__float_as_uint(x), __float_as_uint(x), false, false);
  return __uint_as_float(t[0]) + __uint_as_float(t[1]);
}
__device__ __forceinline__ u32x4 ld_nt(const u16* p) { return __builtin_nontemporal_load(reinterpret_cast<const u32x4*>(p)); }
__device__ __forceinline__ u32x4 ld_pl(const u16* p) { return *reinterpret_cast<const u32x4*>(p); }
__device__ __forceinline__ void put(u64* g, unsigned tag, unsigned v) {
  __hip_atomic_store(g, ((u64)tag << 32) | v, RLX_AGENT);
}
__device__ __forceinline__ u64 get(const u64* g) { return __hip_atomic_load(g, RLX_AGENT); }

#define STAMP(k) do { } while (0)

__global__ void __launch_bounds__(1024, 4)
fused_decode(const u16* __restrict__ xA, const u16* __restrict__ kA, const u16* __restrict__ vA,
             const u16* __restrict__ wqA, const u16* __restrict__ woA,
             const u16* __restrict__ xB, const u16* __restrict__ kB, const u16* __restrict__ vB,
             const u16* __restrict__ wqB, const u16* __restrict__ woB,
             u16* __restrict__ out, u64* ws) {
  __shared__ __attribute__((aligned(16))) char lds[L_END];
  const int tid = threadIdx.x, lane = tid & 63, wave = __builtin_amdgcn_readfirstlane(tid >> 6);
  const int r = lane >> 4, c = lane & 15, tq = c >> 2, tp = c & 3;
  const int bid = blockIdx.x, hkv = bid & 7, j = bid >> 3, p = j >> 4, b = (j >> 2) & 3, s = j & 3, jj = j & 15;
  const u16* x  = p ? xB : xA;
  const u16* kc = p ? kB : kA;
  const u16* vc = p ? vB : vA;
  const u16* wq = p ? wqB : wqA;
  const u16* wo = p ? woB : woA;
  float* qred = reinterpret_cast<float*>(lds + L_QRED);
  unsigned* flag = reinterpret_cast<unsigned*>(lds + L_FLAG);
  float* wmax = reinterpret_cast<float*>(lds + L_WMAX);
  float* lsum = reinterpret_cast<float*>(lds + L_LSUM);
#define TRREAD(byteoff) __builtin_amdgcn_ds_read_tr16_b64_v4i16((lds_s16x4*)(lds + (byteoff)))
#define CAT8(lo, hi) bf16x8{lo[0], lo[1], lo[2], lo[3], hi[0], hi[1], hi[2], hi[3]}

  const unsigned tag = reinterpret_cast<const unsigned*>(ws)[p * 64] + 1u;
  if (tid == 0) __hip_atomic_store(flag, 0u, __ATOMIC_RELAXED, __HIP_MEMORY_SCOPE_WORKGROUP);
  STAMP(0);
  u32x4 wqv[2];
#pragma unroll
  for (int u = 0; u < 2; ++u)
    wqv[u] = ld_nt(wq + (size_t)((jj & 1) * 256 + wave * 16 + u * 8 + (lane >> 3)) * 4096 + hkv * 512 + (jj >> 1) * 64 + (lane & 7) * 8);
  u32x4 xa = ld_pl(x + (c & 3) * 512 + (jj & 1) * 256 + wave * 16 + (r & 1) * 8);
  const size_t kvbase = ((size_t)(b * 8 + hkv) * SK + (SK - WIN) + s * 512 + wave * 32) * DH;
  const u16* kp = kc + kvbase + (size_t)c * DH + r * 8;
  const u16* vp = vc + kvbase + (size_t)r * DH + c * 8;
  const int g_o = j & 3, nq = (j >> 2) & 3, h_o = hkv * 4 + g_o;
  const u16* wop = wo + (size_t)(h_o * 128 + wave * 8 + r) * 512 + nq * 128 + c * 8;
  u32x4 kf[2][4];
  u32x4 vf4[8];
  u32x4 wov[2];
  __syncthreads();
#pragma unroll
  for (int T = 0; T < 2; ++T)
#pragma unroll
    for (int kk = 0; kk < 4; ++kk) kf[T][kk] = ld_nt(kp + T * 16 * DH + kk * 32);
  {
#pragma unroll
    for (int u = 0; u < 2; ++u)
      *reinterpret_cast<u32x4*>(lds + L_IMG + (wave * 16 + u * 8 + (lane >> 3)) * 128 + (lane & 7) * 16) = wqv[u];
    if (c >= 4 || r >= 2) xa = u32x4{0u, 0u, 0u, 0u};
    __builtin_amdgcn_wave_barrier();
#pragma unroll
    for (int ct = 0; ct < 4; ++ct) {
      const s16x4 lo = TRREAD(L_IMG + (wave * 16 + 8 * (r & 1) + tq) * 128 + ct * 32 + tp * 8);
      const s16x4 hi = TRREAD(L_IMG + (wave * 16 + 8 * (r & 1) + 4 + tq) * 128 + ct * 32 + tp * 8);
      const f32x4 d = __builtin_amdgcn_mfma_f32_16x16x32_bf16(__builtin_bit_cast(bf16x8, xa), CAT8(lo, hi), f32x4{0.f, 0.f, 0.f, 0.f}, 0, 0, 0);
      if (lane < 16) {
#pragma unroll
        for (int bb = 0; bb < 4; ++bb) qred[wave * 256 + bb * 64 + ct * 16 + lane] = d[bb];
      }
    }
  }
  STAMP(1);
  __syncthreads();
  if (wave >= 1 && wave <= 4) {
    const int bb = wave - 1;
    float s0 = 0.f;
#pragma unroll
    for (int w = 0; w < NW; ++w) s0 += qred[w * 256 + bb * 64 + lane];
    put(ws + WS_QG + (((size_t)(p * 4 + bb) * 8 + hkv) * 2 + (jj & 1)) * 512 + (jj >> 1) * 64 + lane, tag, __float_as_uint(s0));
  }
  if (wave == 0) {
    const u64* src = ws + WS_QG + ((size_t)(p * 4 + b) * 8 + hkv) * 1024 + lane * 8;
    u64 g0[8], g1[8];
    for (unsigned spins = 0;; ++spins) {
      bool ok = true;
#pragma unroll
      for (int e = 0; e < 8; ++e) { g0[e] = get(src + e); g1[e] = get(src + 512 + e); }
#pragma unroll
      for (int e = 0; e < 8; ++e) ok &= ((unsigned)(g0[e] >> 32) == tag) & ((unsigned)(g1[e] >> 32) == tag);
      if (__all(ok) || spins > SPIN_CAP) break;
    }
    u32x4 qq;
#pragma unroll
    for (int e = 0; e < 4; ++e)
      qq[e] = f2bf(__uint_as_float((unsigned)g0[2 * e]) + __uint_as_float((unsigned)g1[2 * e])) |
              (f2bf(__uint_as_float((unsigned)g0[2 * e + 1]) + __uint_as_float((unsigned)g1[2 * e + 1])) << 16);
    *reinterpret_cast<u32x4*>(lds + L_QS + lane * 16) = qq;
    __builtin_amdgcn_fence(__ATOMIC_RELEASE, "workgroup");
    if (lane == 0) __hip_atomic_store(flag, 1u, __ATOMIC_RELAXED, __HIP_MEMORY_SCOPE_WORKGROUP);
  }
  asm volatile("s_waitcnt vmcnt(0)" ::: "memory");
  STAMP(2);
#pragma unroll
  for (int i = 0; i < 8; ++i) vf4[i] = ld_nt(vp + (size_t)(4 * i) * DH);
#pragma unroll
  for (int u = 0; u < 2; ++u) wov[u] = ld_nt(wop + (size_t)(u * 4) * 512);

  while (__hip_atomic_load(flag, __ATOMIC_RELAXED, __HIP_MEMORY_SCOPE_WORKGROUP) == 0u) __builtin_amdgcn_s_sleep(1);
  __builtin_amdgcn_fence(__ATOMIC_ACQUIRE, "workgroup");
  STAMP(3);
  float t[8];
  {
    bf16x8 qf[4];
#pragma unroll
    for (int kk = 0; kk < 4; ++kk) {
      u32x4 q4 = *reinterpret_cast<const u32x4*>(lds + L_QS + (c & 3) * 256 + kk * 64 + r * 16);
      if (c >= 4) q4 = u32x4{0u, 0u, 0u, 0u};
      qf[kk] = __builtin_bit_cast(bf16x8, q4);
    }
#pragma unroll
    for (int T = 0; T < 2; ++T) {
      f32x4 sc = f32x4{0.f, 0.f, 0.f, 0.f};
#pragma unroll
      for (int kk = 0; kk < 4; ++kk)
        sc = __builtin_amdgcn_mfma_f32_16x16x32_bf16(__builtin_bit_cast(bf16x8, kf[T][kk]), qf[kk], sc, 0, 0, 0);
#pragma unroll
      for (int e = 0; e < 4; ++e) t[T * 4 + e] = bfr(bfr(sc[e]) * 0.08838834764831845f) * 1.4426950408889634f;
    }
    float m = t[0];
#pragma unroll
    for (int e = 1; e < 8; ++e) m = fmaxf(m, t[e]);
    m = xrow16_max(m);
    if (lane < 4) wmax[wave * 4 + lane] = m;
  }
  STAMP(4);
  __syncthreads();
  float M = wmax[c & 3];
#pragma unroll
  for (int w = 1; w < NW; ++w) M = fmaxf(M, wmax[w * 4 + (c & 3)]);
  {
    float l = 0.f;
#pragma unroll
    for (int e = 0; e < 8; ++e) { t[e] = __builtin_amdgcn_exp2f(t[e] - M); l += t[e]; }
    l = xrow16_sum(l);
    if (lane < 4) lsum[wave * 4 + lane] = l;
    if (c < 4) {
#pragma unroll
      for (int T = 0; T < 2; ++T)
        *reinterpret_cast<u32x2*>(lds + L_P + c * 1040 + (wave * 32 + T * 16 + r * 4) * 2) =
            u32x2{f2bf(t[T * 4]) | (f2bf(t[T * 4 + 1]) << 16), f2bf(t[T * 4 + 2]) | (f2bf(t[T * 4 + 3]) << 16)};
    }
#pragma unroll
    for (int i = 0; i < 8; ++i) *reinterpret_cast<u32x4*>(lds + L_IMG + off_b(wave * 32 + 4 * i + r, c)) = vf4[i];
  }
  STAMP(5);
  __syncthreads();
  if (wave < 8) {
    f32x4 o = f32x4{0.f, 0.f, 0.f, 0.f}, o1 = f32x4{0.f, 0.f, 0.f, 0.f};
#pragma unroll
    for (int ks = 0; ks < 16; ++ks) {
      u32x4 pa = *reinterpret_cast<const u32x4*>(lds + L_P + (c & 3) * 1040 + (ks * 32 + r * 8) * 2);
      if (c >= 4) pa = u32x4{0u, 0u, 0u, 0u};
      const s16x4 lo = TRREAD(L_IMG + off_b(ks * 32 + 8 * r + tq, 2 * wave + (tp >> 1)) + 8 * (tp & 1));
      const s16x4 hi = TRREAD(L_IMG + off_b(ks * 32 + 8 * r + 4 + tq, 2 * wave + (tp >> 1)) + 8 * (tp & 1));
      if (ks & 1) o1 = __builtin_amdgcn_mfma_f32_16x16x32_bf16(__builtin_bit_cast(bf16x8, pa), CAT8(lo, hi), o1, 0, 0, 0);
      else o = __builtin_amdgcn_mfma_f32_16x16x32_bf16(__builtin_bit_cast(bf16x8, pa), CAT8(lo, hi), o, 0, 0, 0);
    }
    o += o1;
    const size_t slot = (((size_t)(p * 4 + b) * 8 + hkv) * 4 + s) * 4;
    if (lane < 16) {
#pragma unroll
      for (int g = 0; g < 4; ++g) put(ws + WS_ACC + (slot + g) * 128 + wave * 16 + lane, tag, __float_as_uint(o[g]));
    }
    if (wave == 0 && lane < 4) {
      float L = 0.f;
#pragma unroll
      for (int w = 0; w < NW; ++w) L += lsum[w * 4 + lane];
      put(ws + WS_ML + (slot + lane) * 2, tag, __float_as_uint(M));
      put(ws + WS_ML + (slot + lane) * 2 + 1, tag, __float_as_uint(L));
    }
    STAMP(6);
  } else {
    const int bb = (tid - 512) >> 7, d = tid & 127;
    u64 a[4], mm[4], ll[4];
    for (unsigned spins = 0;; ++spins) {
      bool ok = true;
#pragma unroll
      for (int ss = 0; ss < 4; ++ss) {
        const size_t sl = (((size_t)(p * 4 + bb) * 8 + hkv) * 4 + ss) * 4 + g_o;
        a[ss] = get(ws + WS_ACC + sl * 128 + d); mm[ss] = get(ws + WS_ML + sl * 2); ll[ss] = get(ws + WS_ML + sl * 2 + 1);
      }
#pragma unroll
      for (int ss = 0; ss < 4; ++ss)
        ok &= ((unsigned)(a[ss] >> 32) == tag) & ((unsigned)(mm[ss] >> 32) == tag) & ((unsigned)(ll[ss] >> 32) == tag);
      if (__all(ok) || spins > SPIN_CAP) break;
    }
    STAMP(7);
    float M2 = -INFINITY;
#pragma unroll
    for (int ss = 0; ss < 4; ++ss) M2 = fmaxf(M2, __uint_as_float((unsigned)mm[ss]));
    float L2 = 0.f, o2 = 0.f;
#pragma unroll
    for (int ss = 0; ss < 4; ++ss) {
      const float wt = __builtin_amdgcn_exp2f(__uint_as_float((unsigned)mm[ss]) - M2);
      L2 = fmaf(wt, __uint_as_float((unsigned)ll[ss]), L2); o2 = fmaf(wt, __uint_as_float((unsigned)a[ss]), o2);
    }
    *reinterpret_cast<u16*>(lds + L_OS + bb * 272 + d * 2) = (u16)f2bf(o2 / L2);
  }
  __syncthreads();
#pragma unroll
  for (int u = 0; u < 2; ++u) *reinterpret_cast<u32x4*>(lds + L_IMG + off_b(wave * 8 + u * 4 + r, c)) = wov[u];
  __syncthreads();
  const int idx = jj * 8 + hkv;
  if (wave < 8) {
    f32x4 y = f32x4{0.f, 0.f, 0.f, 0.f};
#pragma unroll
    for (int ks = 0; ks < 4; ++ks) {
      u32x4 oa = *reinterpret_cast<const u32x4*>(lds + L_OS + (c & 3) * 272 + (ks * 32 + r * 8) * 2);
      if (c >= 4) oa = u32x4{0u, 0u, 0u, 0u};
      const s16x4 lo = TRREAD(L_IMG + off_b(ks * 32 + 8 * r + tq, 2 * wave + (tp >> 1)) + 8 * (tp & 1));
      const s16x4 hi = TRREAD(L_IMG + off_b(ks * 32 + 8 * r + 4 + tq, 2 * wave + (tp >> 1)) + 8 * (tp & 1));
      y = __builtin_amdgcn_mfma_f32_16x16x32_bf16(__builtin_bit_cast(bf16x8, oa), CAT8(lo, hi), y, 0, 0, 0);
    }
    if (lane < 16) {
#pragma unroll
      for (int bb = 0; bb < 4; ++bb)
        put(ws + WS_Y + ((size_t)(p * 32 + h_o) * 4 + bb) * 512 + nq * 128 + wave * 16 + lane, tag, __float_as_uint(y[bb]));
    }
    STAMP(8);
  } else {
    const int t2 = tid - 512, br = idx >> 5, n0 = (idx & 31) * 16, hh = t2 & 31, n = t2 >> 5;
    const u64* src = ws + WS_Y + ((size_t)(p * 32 + hh) * 4 + br) * 512 + n0 + n;
    u64 g;
    for (unsigned spins = 0;; ++spins) {
      g = get(src);
      if (__all((unsigned)(g >> 32) == tag) || spins > SPIN_CAP) break;
    }
    STAMP(9);
    float v = __uint_as_float((unsigned)g);
    v += __shfl_xor(v, 1); v += __shfl_xor(v, 2); v += __shfl_xor(v, 4); v += __shfl_xor(v, 8); v += __shfl_xor(v, 16);
    if (hh == 0) out[br * 1024 + p * 512 + n0 + n] = (u16)f2bf(v);
  }
  if (idx == 0) {
    __syncthreads();
    if (tid == 0) __hip_atomic_store(ws + p * 32, (u64)tag, RLX_AGENT);
  }
}

extern "C" void kernel_launch(void* const* d_in, const int* in_sizes, int n_in,
                              void* d_out, int out_size, void* d_ws, size_t ws_size,
                              hipStream_t stream) {
  fused_decode<<<256, 1024, 0, stream>>>((const u16*)d_in[0], (const u16*)d_in[1], (const u16*)d_in[2], (const u16*)d_in[3],
                                        (const u16*)d_in[4], (const u16*)d_in[5], (const u16*)d_in[6], (const u16*)d_in[7],
                                        (const u16*)d_in[8], (const u16*)d_in[9], (u16*)d_out, (u64*)d_ws);
}
```
